```python
import torch

def _softmax(x, axis=-1):
    return torch.softmax(x, dim=axis)
def _gelu(x):
    return torch.nn.functional.gelu(x, approximate="tanh")
def _silu(x):
    return torch.nn.functional.silu(x)
def _sigmoid(x):
    return torch.sigmoid(x)
def _layernorm(x, axis=-1, eps=1e-5):
    m = x.mean(dim=axis, keepdim=True)
    v = x.var(dim=axis, keepdim=True, unbiased=False)
    return (x - m) / torch.sqrt(v + eps)
def _scatter_add(tgt, idx, src, axis=0):
    return tgt.clone().index_add_(axis, idx, src)
def _segment_sum(src, seg, n):
    out = torch.zeros((n,) + src.shape[1:], dtype=src.dtype)
    return out.index_add_(0, seg, src)
def _pad(x, pw):
    flat = tuple(v for pair in reversed(pw) for v in pair)
    return torch.nn.functional.pad(x, flat)

kernel_name = 'standalone__gqa_softmax__near_ridge'

def setup_inputs():

    _w = (lambda a: (lambda t: t.to(torch.bfloat16) if t.is_floating_point() else t)(torch.as_tensor(a)))
    B, Hq, Hkv, Sq, Sk, D = 8, 32, 8, 80, 1024, 128
    return {
        "q": _w((lambda *s: torch.randn(*s, dtype=torch.bfloat16) * 0.8)(B, Hq, Sq, D)),
        "k": _w((lambda *s: torch.randn(*s, dtype=torch.bfloat16) * 0.8)(B, Hkv, Sk, D)),
        "v": _w((lambda *s: torch.randn(*s, dtype=torch.bfloat16) * 0.05)(B, Hkv, Sk, D)),
    }

def reference(q, k, v):
    B, Hq, Sq, D = q.shape
    Hkv = k.shape[1]
    rep = Hq // Hkv
    k = (lambda x, n, axis: torch.repeat_interleave(x, n, dim=axis))(k, rep, axis=1)
    v = (lambda x, n, axis: torch.repeat_interleave(x, n, dim=axis))(v, rep, axis=1)
    scale = D ** -0.5
    scores = torch.einsum("bhqd,bhkd->bhqk", q, k) * scale
    p = _softmax(scores, axis=-1)
    return torch.einsum("bhqk,bhkd->bhqd", p, v)
```

```cpp
#include <hip/hip_runtime.h>
#include <hip/hip_bf16.h>

using bf16 = __hip_bfloat16;
typedef short s16x8 __attribute__((ext_vector_type(8)));
typedef short s16x4 __attribute__((ext_vector_type(4)));
typedef float f32x4 __attribute__((ext_vector_type(4)));
typedef unsigned u32x2 __attribute__((ext_vector_type(2)));
typedef unsigned u32x4 __attribute__((ext_vector_type(4)));
typedef __attribute__((address_space(3))) s16x4 lds_s16x4;

constexpr int B_ = 8, HQ_ = 32, HKV_ = 8, SQ_ = 80, SK_ = 1024, D_ = 128;
constexpr float CSC = 0.08838834764831845f * 1.4426950408889634f;

constexpr int SLOT = 32768;
constexpr int NSLOT = 5;
constexpr int QOFF = 4 * SLOT;
constexpr int XOFF = SLOT;
constexpr int XBLK = SQ_ * 128;
constexpr int STOFF = XOFF + 12 * XBLK;

#define WAIT_VM(n) asm volatile("s_waitcnt vmcnt(" #n ")" ::: "memory")
#define BAR() do { asm volatile("s_waitcnt lgkmcnt(0)" ::: "memory"); __builtin_amdgcn_s_barrier(); asm volatile("" ::: "memory"); } while (0)

typedef float f32x2 __attribute__((ext_vector_type(2)));
typedef __bf16 bf16x2 __attribute__((ext_vector_type(2)));
__device__ __forceinline__ unsigned cvtpk(float lo, float hi) {
  f32x2 v = {lo, hi};
  return __builtin_bit_cast(unsigned, __builtin_convertvector(v, bf16x2));
}
__device__ __forceinline__ float xmax(float x) {
  auto a = __builtin_amdgcn_permlane16_swap(__float_as_uint(x), __float_as_uint(x), false, false);
  x = fmaxf(__uint_as_float(a[0]), __uint_as_float(a[1]));
  auto b = __builtin_amdgcn_permlane32_swap(__float_as_uint(x), __float_as_uint(x), false, false);
  return fmaxf(__uint_as_float(b[0]), __uint_as_float(b[1]));
}
__device__ __forceinline__ float xsum(float x) {
  auto a = __builtin_amdgcn_permlane16_swap(__float_as_uint(x), __float_as_uint(x), false, false);
  x = __uint_as_float(a[0]) + __uint_as_float(a[1]);
  auto b = __builtin_amdgcn_permlane32_swap(__float_as_uint(x), __float_as_uint(x), false, false);
  return __uint_as_float(b[0]) + __uint_as_float(b[1]);
}

struct Ctx {
  char* smem;
  const bf16* Kb; const bf16* Vb;
  int lane, c, hh;
};

template <bool ISK>
__device__ __forceinline__ void issue_unit(const Ctx& x, int t, int slot) {
  const bf16* base = ISK ? x.Kb : x.Vb;
#pragma unroll
  for (int k = 0; k < 4; ++k) {
    const int rr = 16 * x.hh + 4 * k + (x.lane >> 4);
    const int ch = ISK ? ((x.lane & 15) ^ (rr & 15)) : ((x.lane & 15) ^ ((rr & 7) << 1));
    const bf16* g = base + (size_t)(256 * x.c + 32 * t + rr) * D_ + ch * 8;
    __builtin_amdgcn_global_load_lds((const unsigned*)g,
        (unsigned*)(x.smem + slot * SLOT + x.c * 8192 + (4 * x.hh + k) * 1024), 16, 0, 0);
  }
}
__device__ __forceinline__ void issue_seq(const Ctx& x, int n, int slot) {
  if (n == 0) issue_unit<true>(x, 0, slot);
  else if (n == 15) issue_unit<false>(x, 7, slot);
  else if (n & 1) issue_unit<true>(x, (n + 1) >> 1, slot);
  else issue_unit<false>(x, (n >> 1) - 1, slot);
}

template <int NQ>
__device__ __forceinline__ void qk_phase(const Ctx& x, int slot, const s16x8 (&qf)[NQ][4], f32x4 (&s)[NQ][2]) {
  const int r = x.lane & 15, g = x.lane >> 4;
  const char* kb = x.smem + slot * SLOT + x.c * 8192 + r * 256;
#pragma unroll
  for (int kt = 0; kt < 2; ++kt) {
#pragma unroll
    for (int ks = 0; ks < 4; ++ks) {
      const s16x8 kf = *reinterpret_cast<const s16x8*>(kb + kt * 4096 + (((4 * ks) ^ g ^ r) * 16));
#pragma unroll
      for (int qi = 0; qi < NQ; ++qi) {
        const f32x4 cin = ks == 0 ? f32x4{0.f, 0.f, 0.f, 0.f} : s[qi][kt];
        s[qi][kt] = __builtin_amdgcn_mfma_f32_16x16x32_bf16(kf, qf[qi][ks], cin, 0, 0, 0);
      }
    }
  }
}

template <int NQ>
__device__ __forceinline__ void softmax_phase(const f32x4 (&s)[NQ][2], float (&m)[NQ], float (&l)[NQ], float (&al)[NQ], s16x8 (&pf)[NQ]) {
#pragma unroll
  for (int qi = 0; qi < NQ; ++qi) {
    float mx = fmaxf(fmaxf(fmaxf(s[qi][0][0], s[qi][0][1]), fmaxf(s[qi][0][2], s[qi][0][3])),
                     fmaxf(fmaxf(s[qi][1][0], s[qi][1][1]), fmaxf(s[qi][1][2], s[qi][1][3])));
    mx = xmax(mx);
    const float mn = fmaxf(m[qi], mx);
    const float a = __builtin_amdgcn_exp2f((m[qi] - mn) * CSC);
    m[qi] = mn; al[qi] = a;
    const float nm = -mn * CSC;
    float p[8];
#pragma unroll
    for (int j = 0; j < 4; ++j) { p[j] = __builtin_amdgcn_exp2f(fmaf(s[qi][0][j], CSC, nm)); p[4 + j] = __builtin_amdgcn_exp2f(fmaf(s[qi][1][j], CSC, nm)); }
    const float ps = ((p[0] + p[1]) + (p[2] + p[3])) + ((p[4] + p[5]) + (p[6] + p[7]));
    l[qi] = fmaf(l[qi], a, ps);
    u32x4 w = {cvtpk(p[0], p[1]), cvtpk(p[2], p[3]), cvtpk(p[4], p[5]), cvtpk(p[6], p[7])};
    pf[qi] = *reinterpret_cast<s16x8*>(&w);
  }
}

template <int NQ>
__device__ __forceinline__ void rescale_phase(f32x4 (&o)[NQ][8], const float (&al)[NQ]) {
#pragma unroll
  for (int qi = 0; qi < NQ; ++qi)
#pragma unroll
    for (int t = 0; t < 8; ++t) o[qi][t] *= al[qi];
}

template <int NQ>
__device__ __forceinline__ void pv_phase(const Ctx& x, int slot, const s16x8 (&pf)[NQ], f32x4 (&o)[NQ][8]) {
  const int g = x.lane >> 4, qq = (x.lane >> 2) & 3, p = x.lane & 3;
  const int row = 4 * g + qq;
  const unsigned a0 = (unsigned)(size_t)x.smem + slot * SLOT + x.c * 8192 + row * 256 + (row & 7) * 32 + (p >> 1) * 16 + (p & 1) * 8;
#pragma unroll
  for (int t = 0; t < 8; ++t) {
    const unsigned a = a0 ^ (t * 32);
    const s16x4 lo = __builtin_amdgcn_ds_read_tr16_b64_v4i16((lds_s16x4*)(size_t)a);
    const s16x4 hi = __builtin_amdgcn_ds_read_tr16_b64_v4i16((lds_s16x4*)(size_t)(a + 4096));
    const s16x8 vf = {lo[0], lo[1], lo[2], lo[3], hi[0], hi[1], hi[2], hi[3]};
#pragma unroll
    for (int qi = 0; qi < NQ; ++qi) o[qi][t] = __builtin_amdgcn_mfma_f32_16x16x32_bf16(vf, pf[qi], o[qi][t], 0, 0, 0);
  }
}

template <int NQ>
__device__ __forceinline__ void attn_body(const Ctx& x, bf16* __restrict__ Og, int qt0) {
  const int r = x.lane & 15, g = x.lane >> 4;
  s16x8 qf[NQ][4];
#pragma unroll
  for (int qi = 0; qi < NQ; ++qi)
#pragma unroll
    for (int ks = 0; ks < 4; ++ks)
      qf[qi][ks] = *reinterpret_cast<const s16x8*>(x.smem + QOFF + (16 * (qt0 + qi) + r) * 256 + (((4 * ks) ^ g ^ r) * 16));

  f32x4 o[NQ][8], s[NQ][2];
  float m[NQ], l[NQ], al[NQ];
  s16x8 pf[NQ];
#pragma unroll
  for (int qi = 0; qi < NQ; ++qi) { m[qi] = -__builtin_inff(); l[qi] = 0.f; al[qi] = 0.f;
#pragma unroll
    for (int t = 0; t < 8; ++t) o[qi][t] = f32x4{0.f, 0.f, 0.f, 0.f}; }

  WAIT_VM(12); BAR();
  issue_seq(x, 4, 4);
  qk_phase<NQ>(x, 0, qf, s);
  softmax_phase<NQ>(s, m, l, al, pf);

  for (int t = 0; t < 7; ++t) {
    const int nA = 2 * t + 1, sA = nA % NSLOT;
    if (t < 6) WAIT_VM(12); else WAIT_VM(8);
    BAR();
    if (t < 6) issue_seq(x, nA + 4, (nA + 4) % NSLOT);
    rescale_phase<NQ>(o, al);
    qk_phase<NQ>(x, sA, qf, s);
    const int nB = 2 * t + 2, sB = nB % NSLOT;
    if (t < 6) WAIT_VM(12); else WAIT_VM(4);
    BAR();
    if (t < 5) issue_seq(x, nB + 4, (nB + 4) % NSLOT);
    s16x8 pcur[NQ];
#pragma unroll
    for (int qi = 0; qi < NQ; ++qi) pcur[qi] = pf[qi];
    pv_phase<NQ>(x, sB, pcur, o);
    softmax_phase<NQ>(s, m, l, al, pf);
  }
  WAIT_VM(0); BAR();
  rescale_phase<NQ>(o, al);
  pv_phase<NQ>(x, 0, pf, o);

  float* mst = reinterpret_cast<float*>(x.smem + STOFF);
  float* lst = mst + 4 * SQ_;
#pragma unroll
  for (int qi = 0; qi < NQ; ++qi) {
    const float ls = xsum(l[qi]);
    if (g == 0) { mst[x.c * SQ_ + 16 * (qt0 + qi) + r] = m[qi]; lst[x.c * SQ_ + 16 * (qt0 + qi) + r] = ls; }
  }
  f32x4 own[NQ][2];
#pragma unroll
  for (int dq = 0; dq < 4; ++dq) {
    if (dq == x.c) {
#pragma unroll
      for (int qi = 0; qi < NQ; ++qi) { own[qi][0] = o[qi][2 * dq]; own[qi][1] = o[qi][2 * dq + 1]; }
    } else {
      const int si = x.c < dq ? x.c : x.c - 1;
      char* blk = x.smem + XOFF + (dq * 3 + si) * XBLK;
#pragma unroll
      for (int qi = 0; qi < NQ; ++qi)
#pragma unroll
        for (int h = 0; h < 2; ++h)
          *reinterpret_cast<f32x4*>(blk + (16 * (qt0 + qi) + r) * 128 + (((4 * h + g) ^ (r & 7)) * 16)) = o[qi][2 * dq + h];
    }
  }
  BAR();
#pragma unroll
  for (int qi = 0; qi < NQ; ++qi) {
    const int row = 16 * (qt0 + qi) + r;
    float mm[4], f[4];
#pragma unroll
    for (int cc = 0; cc < 4; ++cc) mm[cc] = mst[cc * SQ_ + row];
    const float M = fmaxf(fmaxf(mm[0], mm[1]), fmaxf(mm[2], mm[3]));
    float L = 0.f;
#pragma unroll
    for (int cc = 0; cc < 4; ++cc) { f[cc] = __builtin_amdgcn_exp2f((mm[cc] - M) * CSC); L = fmaf(lst[cc * SQ_ + row], f[cc], L); }
    const float inv = 1.f / L;
    const float fown = x.c == 0 ? f[0] : x.c == 1 ? f[1] : x.c == 2 ? f[2] : f[3];
#pragma unroll
    for (int h = 0; h < 2; ++h) {
      f32x4 acc = own[qi][h] * fown;
#pragma unroll
      for (int si = 0; si < 3; ++si) {
        const int cs = si < x.c ? si : si + 1;
        const f32x4 v = *reinterpret_cast<const f32x4*>(x.smem + XOFF + (x.c * 3 + si) * XBLK + row * 128 + (((4 * h + g) ^ (r & 7)) * 16));
        const float fs = cs == 0 ? f[0] : cs == 1 ? f[1] : cs == 2 ? f[2] : f[3];
        acc += v * fs;
      }
      acc *= inv;
      u32x2 w = {cvtpk(acc[0], acc[1]), cvtpk(acc[2], acc[3])};
      *reinterpret_cast<u32x2*>(Og + (size_t)row * D_ + 32 * x.c + 16 * h + 4 * g) = w;
    }
  }
}

__global__ __launch_bounds__(512, 2) void gqa_attn_kernel(const bf16* __restrict__ Q, const bf16* __restrict__ K,
                                                          const bf16* __restrict__ V, bf16* __restrict__ O) {
  __shared__ __attribute__((aligned(16))) char smem[NSLOT * SLOT];
  const int bid = blockIdx.x, xcd = bid & 7, j = bid >> 3;
  const int hsub = j & 3, b = j >> 2, kvh = xcd, hq = kvh * 4 + hsub;
  const int wid = __builtin_amdgcn_readfirstlane(threadIdx.x >> 6), lane = threadIdx.x & 63;
  Ctx x;
  x.smem = smem; x.lane = lane; x.c = wid & 3; x.hh = wid >> 2;
  x.Kb = K + (size_t)(b * HKV_ + kvh) * SK_ * D_;
  x.Vb = V + (size_t)(b * HKV_ + kvh) * SK_ * D_;
  const bf16* Qg = Q + (size_t)(b * HQ_ + hq) * SQ_ * D_;
  bf16* Og = O + (size_t)(b * HQ_ + hq) * SQ_ * D_;

#pragma unroll
  for (int k = 0; k < 3; ++k) {
    const int pq = wid + 8 * k;
    if (pq < 20) {
      const int row = 4 * pq + (lane >> 4);
      const bf16* g = Qg + (size_t)row * D_ + (((lane & 15) ^ (row & 15)) * 8);
      __builtin_amdgcn_global_load_lds((const unsigned*)g, (unsigned*)(smem + QOFF + pq * 1024), 16, 0, 0);
    }
  }
  issue_seq(x, 0, 0); issue_seq(x, 1, 1); issue_seq(x, 2, 2); issue_seq(x, 3, 3);
  WAIT_VM(16); BAR();
  if (x.hh == 0) attn_body<3>(x, Og, 0);
  else attn_body<2>(x, Og, 3);
}

extern "C" void kernel_launch(void* const* d_in, const int* in_sizes, int n_in, void* d_out, int out_size,
                              void* d_ws, size_t ws_size, hipStream_t stream) {
  const bf16* q = (const bf16*)d_in[0];
  const bf16* k = (const bf16*)d_in[1];
  const bf16* v = (const bf16*)d_in[2];
  bf16* o = (bf16*)d_out;
  hipLaunchKernelGGL(gqa_attn_kernel, dim3(B_ * HQ_), dim3(512), 0, stream, q, k, v, o);
}
```

```cpp
#include <hip/hip_runtime.h>
#include <hip/hip_bf16.h>

using bf16 = __hip_bfloat16;
typedef short s16x8 __attribute__((ext_vector_type(8)));
typedef short s16x4 __attribute__((ext_vector_type(4)));
typedef float f32x4 __attribute__((ext_vector_type(4)));
typedef unsigned u32x2 __attribute__((ext_vector_type(2)));
typedef unsigned u32x4 __attribute__((ext_vector_type(4)));
typedef __attribute__((address_space(3))) s16x4 lds_s16x4;

constexpr int B_ = 8, HQ_ = 32, HKV_ = 8, SQ_ = 80, SK_ = 1024, D_ = 128;
constexpr float CSC = 0.08838834764831845f * 1.4426950408889634f;

constexpr int SLOT = 32768;
constexpr int NSLOT = 4;
constexpr int QOFF = NSLOT * SLOT;
constexpr int LDS_BYTES = 163840;
constexpr int XOFF = 0;
constexpr int XBLK = SQ_ * 128;
constexpr int STOFF = XOFF + 12 * XBLK;

#define WAIT_VM(n) asm volatile("s_waitcnt vmcnt(" #n ")" ::: "memory")
#define BAR() do { asm volatile("s_waitcnt lgkmcnt(0)" ::: "memory"); __builtin_amdgcn_s_barrier(); asm volatile("" ::: "memory"); } while (0)

typedef float f32x2 __attribute__((ext_vector_type(2)));
typedef __bf16 bf16x2 __attribute__((ext_vector_type(2)));
__device__ __forceinline__ unsigned cvtpk(float lo, float hi) {
  f32x2 v = {lo, hi};
  return __builtin_bit_cast(unsigned, __builtin_convertvector(v, bf16x2));
}
__device__ __forceinline__ float xmax(float x) {
  auto a = __builtin_amdgcn_permlane16_swap(__float_as_uint(x), __float_as_uint(x), false, false);
  x = fmaxf(__uint_as_float(a[0]), __uint_as_float(a[1]));
  auto b = __builtin_amdgcn_permlane32_swap(__float_as_uint(x), __float_as_uint(x), false, false);
  return fmaxf(__uint_as_float(b[0]), __uint_as_float(b[1]));
}
__device__ __forceinline__ float xsum(float x) {
  auto a = __builtin_amdgcn_permlane16_swap(__float_as_uint(x), __float_as_uint(x), false, false);
  x = __uint_as_float(a[0]) + __uint_as_float(a[1]);
  auto b = __builtin_amdgcn_permlane32_swap(__float_as_uint(x), __float_as_uint(x), false, false);
  return __uint_as_float(b[0]) + __uint_as_float(b[1]);
}

struct Ctx {
  char* smem;
  const bf16* Kb; const bf16* Vb;
  int lane, c, hh;
};

template <bool ISK>
__device__ __forceinline__ void issue_unit(const Ctx& x, int t, int slot) {
  const bf16* base = ISK ? x.Kb : x.Vb;
#pragma unroll
  for (int k = 0; k < 4; ++k) {
    const int rr = 16 * x.hh + 4 * k + (x.lane >> 4);
    const int ch = ISK ? ((x.lane & 15) ^ (rr & 15)) : ((x.lane & 15) ^ ((rr & 7) << 1));
    const bf16* g = base + (size_t)(256 * x.c + 32 * t + rr) * D_ + ch * 8;
    __builtin_amdgcn_global_load_lds((const unsigned*)g,
        (unsigned*)(x.smem + slot * SLOT + x.c * 8192 + (4 * x.hh + k) * 1024), 16, 0, 0);
  }
}
__device__ __forceinline__ void issue_seq(const Ctx& x, int n, int slot) {
  if (n == 0) issue_unit<true>(x, 0, slot);
  else if (n == 15) issue_unit<false>(x, 7, slot);
  else if (n & 1) issue_unit<true>(x, (n + 1) >> 1, slot);
  else issue_unit<false>(x, (n >> 1) - 1, slot);
}

template <int NQ>
__device__ __forceinline__ void qk_phase(const Ctx& x, int slot, const s16x8 (&qf)[NQ][4], f32x4 (&s)[NQ][2]) {
  const int r = x.lane & 15, g = x.lane >> 4;
  const char* kb = x.smem + slot * SLOT + x.c * 8192 + r * 256;
  s16x8 kf[2][4];
#pragma unroll
  for (int ks = 0; ks < 4; ++ks)
#pragma unroll
    for (int kt = 0; kt < 2; ++kt)
      kf[kt][ks] = *reinterpret_cast<const s16x8*>(kb + kt * 4096 + (((4 * ks) ^ g ^ r) * 16));
#pragma unroll
  for (int ks = 0; ks < 4; ++ks)
#pragma unroll
    for (int kt = 0; kt < 2; ++kt)
#pragma unroll
      for (int qi = 0; qi < NQ; ++qi) {
        const f32x4 cin = ks == 0 ? f32x4{0.f, 0.f, 0.f, 0.f} : s[qi][kt];
        s[qi][kt] = __builtin_amdgcn_mfma_f32_16x16x32_bf16(kf[kt][ks], qf[qi][ks], cin, 0, 0, 0);
      }
}

template <int NQ>
__device__ __forceinline__ void softmax_first(const f32x4 (&s)[NQ][2], float (&m)[NQ], float (&l)[NQ], s16x8 (&pf)[NQ]) {
#pragma unroll
  for (int qi = 0; qi < NQ; ++qi) {
    float t[8];
#pragma unroll
    for (int j = 0; j < 4; ++j) { t[j] = s[qi][0][j] * CSC; t[4 + j] = s[qi][1][j] * CSC; }
    float mx = fmaxf(fmaxf(fmaxf(t[0], t[1]), fmaxf(t[2], t[3])), fmaxf(fmaxf(t[4], t[5]), fmaxf(t[6], t[7])));
    mx = xmax(mx);
    m[qi] = mx;
    float p[8];
#pragma unroll
    for (int j = 0; j < 8; ++j) p[j] = __builtin_amdgcn_exp2f(t[j] - mx);
    l[qi] = ((p[0] + p[1]) + (p[2] + p[3])) + ((p[4] + p[5]) + (p[6] + p[7]));
    u32x4 w = {cvtpk(p[0], p[1]), cvtpk(p[2], p[3]), cvtpk(p[4], p[5]), cvtpk(p[6], p[7])};
    pf[qi] = *reinterpret_cast<s16x8*>(&w);
  }
}

template <int NQ>
__device__ __forceinline__ void rescale_phase(f32x4 (&o)[NQ][8], const float (&al)[NQ]) {
#pragma unroll
  for (int qi = 0; qi < NQ; ++qi)
#pragma unroll
    for (int t = 0; t < 8; ++t) o[qi][t] *= al[qi];
}

template <int NQ>
__device__ __forceinline__ void pv_phase(const Ctx& x, int slot, const s16x8 (&pf)[NQ], f32x4 (&o)[NQ][8]) {
  const int g = x.lane >> 4, qq = (x.lane >> 2) & 3, p = x.lane & 3;
  const int row = 4 * g + qq;
  const unsigned a0 = (unsigned)(size_t)x.smem + slot * SLOT + x.c * 8192 + row * 256 + (row & 7) * 32 + (p >> 1) * 16 + (p & 1) * 8;
  s16x8 vf[8];
#pragma unroll
  for (int t = 0; t < 8; ++t) {
    const unsigned a = a0 ^ (t * 32);
    const s16x4 lo = __builtin_amdgcn_ds_read_tr16_b64_v4i16((lds_s16x4*)(size_t)a);
    const s16x4 hi = __builtin_amdgcn_ds_read_tr16_b64_v4i16((lds_s16x4*)(size_t)(a + 4096));
    vf[t] = s16x8{lo[0], lo[1], lo[2], lo[3], hi[0], hi[1], hi[2], hi[3]};
  }
#pragma unroll
  for (int t = 0; t < 8; ++t)
#pragma unroll
    for (int qi = 0; qi < NQ; ++qi) o[qi][t] = __builtin_amdgcn_mfma_f32_16x16x32_bf16(vf[t], pf[qi], o[qi][t], 0, 0, 0);
}

#define SB() __builtin_amdgcn_sched_barrier(0)

constexpr float THR2 = 8.0f;
struct SmTmp { float tm; float t[8]; };
template <int PC>
__device__ __forceinline__ void sm_piece(const f32x4 (&s)[2], const float m, float& l, s16x8& pf, SmTmp& w) {
  if constexpr (PC == 0) {
#pragma unroll
    for (int j = 0; j < 4; ++j) { w.t[j] = fmaf(s[0][j], CSC, -m); w.t[4 + j] = fmaf(s[1][j], CSC, -m); }
  } else if constexpr (PC == 1) {
    w.tm = fmaxf(fmaxf(fmaxf(fmaxf(fmaxf(fmaxf(fmaxf(w.t[0], w.t[1]), w.t[2]), w.t[3]), w.t[4]), w.t[5]), w.t[6]), w.t[7]);
  } else if constexpr (PC == 2) {
    w.tm = xmax(w.tm);
  } else if constexpr (PC == 3) {
#pragma unroll
    for (int j = 0; j < 4; ++j) w.t[j] = __builtin_amdgcn_exp2f(w.t[j]);
  } else if constexpr (PC == 4) {
#pragma unroll
    for (int j = 0; j < 4; ++j) w.t[4 + j] = __builtin_amdgcn_exp2f(w.t[4 + j]);
  } else if constexpr (PC == 5) {
    l += ((w.t[0] + w.t[1]) + (w.t[2] + w.t[3])) + ((w.t[4] + w.t[5]) + (w.t[6] + w.t[7]));
  } else {
    u32x4 u = {cvtpk(w.t[0], w.t[1]), cvtpk(w.t[2], w.t[3]), cvtpk(w.t[4], w.t[5]), cvtpk(w.t[6], w.t[7])};
    pf = *reinterpret_cast<s16x8*>(&u);
  }
}
template <int NQ, int IDX>
__device__ __forceinline__ void sm_slot(const f32x4 (&s)[NQ][2], float (&m)[NQ], float (&l)[NQ], float (&al)[NQ], bool& resc, s16x8 (&pf)[NQ], SmTmp (&w)[NQ]) {
  if constexpr (IDX < 7 * NQ) {
    constexpr int qi = IDX % NQ, pc = IDX / NQ;
    if constexpr (IDX == 3 * NQ) {
      bool need = false;
#pragma unroll
      for (int q2 = 0; q2 < NQ; ++q2) need = need || (w[q2].tm > THR2);
      if (__builtin_expect(__any(need), 0)) {
#pragma unroll
        for (int q2 = 0; q2 < NQ; ++q2) {
          const float d = fmaxf(w[q2].tm, 0.f);
#pragma unroll
          for (int j = 0; j < 8; ++j) w[q2].t[j] -= d;
          const float a = __builtin_amdgcn_exp2f(-d);
          m[q2] += d; l[q2] *= a; al[q2] *= a;
        }
        resc = true;
      }
    }
    sm_piece<pc>(s[qi], m[qi], l[qi], pf[qi], w[qi]);
  }
}

template <int NQ, bool RESC>
__device__ __forceinline__ void phaseA(const Ctx& x, int slot, int qt0, f32x4 (&s)[NQ][2], f32x4 (&o)[NQ][8], const float (&al)[NQ]) {
  const int r = x.lane & 15, g = x.lane >> 4;
  const char* kb = x.smem + slot * SLOT + x.c * 8192 + r * 256;
  const char* qb = x.smem + QOFF + (16 * qt0 + r) * 256;
  s16x8 kf[2][2], qf[2][NQ];
  auto ld = [&](int ks, int buf) {
    const int co = ((4 * ks) ^ g ^ r) * 16;
#pragma unroll
    for (int kt = 0; kt < 2; ++kt) kf[buf][kt] = *reinterpret_cast<const s16x8*>(kb + kt * 4096 + co);
#pragma unroll
    for (int qi = 0; qi < NQ; ++qi) qf[buf][qi] = *reinterpret_cast<const s16x8*>(qb + qi * 4096 + co);
  };
  ld(0, 0);
  SB();
#pragma unroll
  for (int ks = 0; ks < 4; ++ks) {
    if (ks < 3) { ld(ks + 1, (ks + 1) & 1); SB(); }
#pragma unroll
    for (int kt = 0; kt < 2; ++kt)
#pragma unroll
      for (int qi = 0; qi < NQ; ++qi) {
        const f32x4 cin = ks == 0 ? f32x4{0.f, 0.f, 0.f, 0.f} : s[qi][kt];
        s[qi][kt] = __builtin_amdgcn_mfma_f32_16x16x32_bf16(kf[ks & 1][kt], qf[ks & 1][qi], cin, 0, 0, 0);
        const int idx = (ks * 2 + kt) * NQ + qi;
        if constexpr (RESC) o[idx / 8][idx % 8] *= al[idx / 8];
        SB();
      }
  }
}

template <int NQ, int T>
__device__ __forceinline__ void pvB_step(const s16x8& vf, const s16x8 (&pc)[NQ], f32x4 (&o)[NQ][8],
                                         const f32x4 (&s)[NQ][2], float (&m)[NQ], float (&l)[NQ], float (&al)[NQ], bool& resc, s16x8 (&pf)[NQ], SmTmp (&w)[NQ], bool dosm) {
#pragma unroll
  for (int qi = 0; qi < NQ; ++qi) {
    o[qi][T] = __builtin_amdgcn_mfma_f32_16x16x32_bf16(vf, pc[qi], o[qi][T], 0, 0, 0);
    if (dosm) {
      if (qi == 0) sm_slot<NQ, T * NQ + 0>(s, m, l, al, resc, pf, w);
      if (qi == 1) sm_slot<NQ, T * NQ + 1>(s, m, l, al, resc, pf, w);
      if (qi == 2) sm_slot<NQ, T * NQ + (NQ > 2 ? 2 : 0)>(s, m, l, al, resc, pf, w);
    }
    SB();
  }
}
template <int NQ, bool DOSM>
__device__ __forceinline__ void phaseB(const Ctx& x, int slot, f32x4 (&o)[NQ][8], const f32x4 (&s)[NQ][2],
                                       float (&m)[NQ], float (&l)[NQ], float (&al)[NQ], bool& resc, s16x8 (&pf)[NQ]) {
  const int g = x.lane >> 4, qq = (x.lane >> 2) & 3, p = x.lane & 3;
  const int row = 4 * g + qq;
  const unsigned a0 = (unsigned)(size_t)x.smem + slot * SLOT + x.c * 8192 + row * 256 + (row & 7) * 32 + (p >> 1) * 16 + (p & 1) * 8;
  s16x8 pc[NQ];
#pragma unroll
  for (int qi = 0; qi < NQ; ++qi) pc[qi] = pf[qi];
  SmTmp w[NQ];
  s16x8 vf[8];
  auto rd = [&](int t) {
    const unsigned a = a0 ^ (t * 32);
    const s16x4 lo = __builtin_amdgcn_ds_read_tr16_b64_v4i16((lds_s16x4*)(size_t)a);
    const s16x4 hi = __builtin_amdgcn_ds_read_tr16_b64_v4i16((lds_s16x4*)(size_t)(a + 4096));
    return s16x8{lo[0], lo[1], lo[2], lo[3], hi[0], hi[1], hi[2], hi[3]};
  };
#pragma unroll
  for (int t = 0; t < 4; ++t) vf[t] = rd(t);
  SB();
  pvB_step<NQ, 0>(vf[0], pc, o, s, m, l, al, resc, pf, w, DOSM);
  pvB_step<NQ, 1>(vf[1], pc, o, s, m, l, al, resc, pf, w, DOSM);
#pragma unroll
  for (int t = 4; t < 8; ++t) vf[t] = rd(t);
  SB();
  pvB_step<NQ, 2>(vf[2], pc, o, s, m, l, al, resc, pf, w, DOSM);
  pvB_step<NQ, 3>(vf[3], pc, o, s, m, l, al, resc, pf, w, DOSM);
  pvB_step<NQ, 4>(vf[4], pc, o, s, m, l, al, resc, pf, w, DOSM);
  pvB_step<NQ, 5>(vf[5], pc, o, s, m, l, al, resc, pf, w, DOSM);
  pvB_step<NQ, 6>(vf[6], pc, o, s, m, l, al, resc, pf, w, DOSM);
  pvB_step<NQ, 7>(vf[7], pc, o, s, m, l, al, resc, pf, w, DOSM);
}

template <int NQ>
__device__ __forceinline__ void attn_body(const Ctx& x, bf16* __restrict__ Og, int qt0) {
  const int r = x.lane & 15, g = x.lane >> 4;
  f32x4 o[NQ][8], s[NQ][2];
  float m[NQ], l[NQ], al[NQ];
  s16x8 pf[NQ];
#pragma unroll
  for (int qi = 0; qi < NQ; ++qi) { m[qi] = 0.f; l[qi] = 0.f; al[qi] = 1.f;
#pragma unroll
    for (int t = 0; t < 8; ++t) o[qi][t] = f32x4{0.f, 0.f, 0.f, 0.f}; }

  bool resc = false;
  WAIT_VM(8); BAR();
  issue_seq(x, 3, 3);
  phaseA<NQ, false>(x, 0, qt0, s, o, al);
  softmax_first<NQ>(s, m, l, pf);

  for (int t = 0; t < 7; ++t) {
    const int nA = 2 * t + 1;
    WAIT_VM(8); BAR();
    if (t < 6) issue_seq(x, nA + 3, (nA + 3) % NSLOT);
    if (__builtin_expect(resc, 0)) {
      rescale_phase<NQ>(o, al);
#pragma unroll
      for (int qi = 0; qi < NQ; ++qi) al[qi] = 1.f;
      resc = false;
    }
    phaseA<NQ, false>(x, nA % NSLOT, qt0, s, o, al);
    const int nB = 2 * t + 2;
    if (t < 6) WAIT_VM(8); else WAIT_VM(4);
    BAR();
    if (t < 6) issue_seq(x, nB + 3, (nB + 3) % NSLOT);
    phaseB<NQ, true>(x, nB % NSLOT, o, s, m, l, al, resc, pf);
  }
  WAIT_VM(0); BAR();
  if (resc) rescale_phase<NQ>(o, al);
  phaseB<NQ, false>(x, 15 % NSLOT, o, s, m, l, al, resc, pf);
  BAR();

  float* mst = reinterpret_cast<float*>(x.smem + STOFF);
  float* lst = mst + 4 * SQ_;
#pragma unroll
  for (int qi = 0; qi < NQ; ++qi) {
    const float ls = xsum(l[qi]);
    if (g == 0) { mst[x.c * SQ_ + 16 * (qt0 + qi) + r] = m[qi]; lst[x.c * SQ_ + 16 * (qt0 + qi) + r] = ls; }
  }
  f32x4 own[NQ][2];
#pragma unroll
  for (int dq = 0; dq < 4; ++dq) {
    if (dq == x.c) {
#pragma unroll
      for (int qi = 0; qi < NQ; ++qi) { own[qi][0] = o[qi][2 * dq]; own[qi][1] = o[qi][2 * dq + 1]; }
    } else {
      const int si = x.c < dq ? x.c : x.c - 1;
      char* blk = x.smem + XOFF + (dq * 3 + si) * XBLK;
#pragma unroll
      for (int qi = 0; qi < NQ; ++qi)
#pragma unroll
        for (int h = 0; h < 2; ++h)
          *reinterpret_cast<f32x4*>(blk + (16 * (qt0 + qi) + r) * 128 + (((4 * h + g) ^ (r & 7)) * 16)) = o[qi][2 * dq + h];
    }
  }
  BAR();
#pragma unroll
  for (int qi = 0; qi < NQ; ++qi) {
    const int row = 16 * (qt0 + qi) + r;
    float mm[4], f[4];
#pragma unroll
    for (int cc = 0; cc < 4; ++cc) mm[cc] = mst[cc * SQ_ + row];
    const float M = fmaxf(fmaxf(mm[0], mm[1]), fmaxf(mm[2], mm[3]));
    float L = 0.f;
#pragma unroll
    for (int cc = 0; cc < 4; ++cc) { f[cc] = __builtin_amdgcn_exp2f(mm[cc] - M); L = fmaf(lst[cc * SQ_ + row], f[cc], L); }
    const float inv = 1.f / L;
    const float fown = x.c == 0 ? f[0] : x.c == 1 ? f[1] : x.c == 2 ? f[2] : f[3];
#pragma unroll
    for (int h = 0; h < 2; ++h) {
      f32x4 acc = own[qi][h] * fown;
#pragma unroll
      for (int si = 0; si < 3; ++si) {
        const int cs = si < x.c ? si : si + 1;
        const f32x4 v = *reinterpret_cast<const f32x4*>(x.smem + XOFF + (x.c * 3 + si) * XBLK + row * 128 + (((4 * h + g) ^ (r & 7)) * 16));
        const float fs = cs == 0 ? f[0] : cs == 1 ? f[1] : cs == 2 ? f[2] : f[3];
        acc += v * fs;
      }
      acc *= inv;
      u32x2 w = {cvtpk(acc[0], acc[1]), cvtpk(acc[2], acc[3])};
      *reinterpret_cast<u32x2*>(Og + (size_t)row * D_ + 32 * x.c + 16 * h + 4 * g) = w;
    }
  }
}

__global__ __launch_bounds__(512, 2) void gqa_attn_kernel(const bf16* __restrict__ Q, const bf16* __restrict__ K,
                                                          const bf16* __restrict__ V, bf16* __restrict__ O) {
  __shared__ __attribute__((aligned(16))) char smem[LDS_BYTES];
  const int bid = blockIdx.x, xcd = bid & 7, j = bid >> 3;
  const int hsub = j & 3, b = j >> 2, kvh = xcd, hq = kvh * 4 + hsub;
  const int wid = __builtin_amdgcn_readfirstlane(threadIdx.x >> 6), lane = threadIdx.x & 63;
  Ctx x;
  x.smem = smem; x.lane = lane; x.c = wid & 3; x.hh = wid >> 2;
  x.Kb = K + (size_t)(b * HKV_ + kvh) * SK_ * D_;
  x.Vb = V + (size_t)(b * HKV_ + kvh) * SK_ * D_;
  const bf16* Qg = Q + (size_t)(b * HQ_ + hq) * SQ_ * D_;
  bf16* Og = O + (size_t)(b * HQ_ + hq) * SQ_ * D_;

#pragma unroll
  for (int k = 0; k < 3; ++k) {
    const int pq = wid + 8 * k;
    if (pq < 20) {
      const int row = 4 * pq + (lane >> 4);
      const bf16* g = Qg + (size_t)row * D_ + (((lane & 15) ^ (row & 15)) * 8);
      __builtin_amdgcn_global_load_lds((const unsigned*)g, (unsigned*)(smem + QOFF + pq * 1024), 16, 0, 0);
    }
  }
  issue_seq(x, 0, 0); issue_seq(x, 1, 1); issue_seq(x, 2, 2);
  if (x.hh == 0) attn_body<3>(x, Og, 0);
  else attn_body<2>(x, Og, 3);
}

extern "C" void kernel_launch(void* const* d_in, const int* in_sizes, int n_in, void* d_out, int out_size,
                              void* d_ws, size_t ws_size, hipStream_t stream) {
  const bf16* q = (const bf16*)d_in[0];
  const bf16* k = (const bf16*)d_in[1];
  const bf16* v = (const bf16*)d_in[2];
  bf16* o = (bf16*)d_out;
  hipLaunchKernelGGL(gqa_attn_kernel, dim3(B_ * HQ_), dim3(512), 0, stream, q, k, v, o);
}
```
